```python
import torch

def _softmax(x, axis=-1):
    return torch.softmax(x, dim=axis)
def _gelu(x):
    return torch.nn.functional.gelu(x, approximate="tanh")
def _silu(x):
    return torch.nn.functional.silu(x)
def _sigmoid(x):
    return torch.sigmoid(x)
def _layernorm(x, axis=-1, eps=1e-5):
    m = x.mean(dim=axis, keepdim=True)
    v = x.var(dim=axis, keepdim=True, unbiased=False)
    return (x - m) / torch.sqrt(v + eps)
def _scatter_add(tgt, idx, src, axis=0):
    return tgt.clone().index_add_(axis, idx, src)
def _segment_sum(src, seg, n):
    out = torch.zeros((n,) + src.shape[1:], dtype=src.dtype)
    return out.index_add_(0, seg, src)
def _pad(x, pw):
    flat = tuple(v for pair in reversed(pw) for v in pair)
    return torch.nn.functional.pad(x, flat)

kernel_name = 'sequential_preLN__linear_attn_block__dense_activation_silu__prompt'

def setup_inputs():

    _w = (lambda a: (lambda t: t.to(torch.bfloat16) if t.is_floating_point() else t)(torch.as_tensor(a)))
    def _A_setup():
        B, S, D = 1, 2048, 512
        return {
            "x": _w((lambda *s: torch.randn(*s, dtype=torch.bfloat16) * 0.05)(B, S, D)),
            "wqkv": _w((lambda *s: torch.randn(*s, dtype=torch.bfloat16) * 0.05)(D, 3 * D)),
            "wo": _w((lambda *s: torch.randn(*s, dtype=torch.bfloat16) * 0.05)(D, D)),
        }
    _A_in = _A_setup()

    def _B_setup():
        B, S, D, F = 1, 2048, 512, 1792
        return {
            "x": _w((lambda *s: torch.randn(*s, dtype=torch.bfloat16) * 0.05)(B, S, D)),
            "w1": _w((lambda *s: torch.randn(*s, dtype=torch.bfloat16) * 0.8)(D, F) * (D ** -0.5)),
            "w2": _w((lambda *s: torch.randn(*s, dtype=torch.bfloat16) * 0.1)(F, D) * (F ** -0.5)),
        }
    _B_in = _B_setup()

    return {
        "A_x": _A_in["x"],
        "A_wqkv": _A_in["wqkv"],
        "A_wo": _A_in["wo"],
        "B_x": _B_in["x"],
        "B_w1": _B_in["w1"],
        "B_w2": _B_in["w2"],
    }

def reference(A_x, A_wqkv, A_wo, B_x, B_w1, B_w2):
    def _A_ref(x, wqkv, wo):
        B, S, D = x.shape
        H = 32; Dh = D // H
        qkv = (lambda x, *d: x.permute(*d))((x @ wqkv).reshape(B, S, 3, H, Dh), 2, 0, 3, 1, 4)
        q, k, v = qkv[0], qkv[1], qkv[2]
        phi = lambda t: torch.where(t > 0, t + 1, torch.exp(t)) + 1e-6
        pq, pk = phi(q), phi(k)
        kv = torch.einsum("bhsd,bhse->bhde", pk, v)
        z = torch.einsum("bhsd->bhd", pk)
        num = torch.einsum("bhqd,bhde->bhqe", pq, kv)
        den = torch.einsum("bhqd,bhd->bhq", pq, z)[..., None] + 1e-6
        o = num / den
        return (lambda x, *d: x.permute(*d))(o, 0, 2, 1, 3).reshape(B, S, D) @ wo

    def _B_ref(x, w1, w2):
        h = torch.einsum("bsd,df->bsf", x, w1)
        h = _silu(h)
        out = torch.einsum("bsf,fd->bsd", h, w2)
        return out

    x = A_x
    h = x + _A_ref(_layernorm(x, axis=-1), A_wqkv, A_wo)
    h2 = h + _B_ref(_layernorm(h, axis=-1), B_w1, B_w2)
    return h2
```

```cpp
#include <hip/hip_runtime.h>
#include <hip/hip_bf16.h>


using bf16x8 = __attribute__((ext_vector_type(8))) short;
using s16x4  = __attribute__((ext_vector_type(4))) short;
using f32x4  = __attribute__((ext_vector_type(4))) float;
using u32x4  = __attribute__((ext_vector_type(4))) unsigned;

#define S_TOK 2048
#define DM 512
#define FF 1792

__device__ __forceinline__ float bf2f(unsigned short v) { return __uint_as_float(((unsigned)v) << 16); }
__device__ __forceinline__ unsigned short f2bf(float f) {
  __hip_bfloat16 b = __float2bfloat16(f);
  return *reinterpret_cast<unsigned short*>(&b);
}
__device__ __forceinline__ float lo16(unsigned u) { return __uint_as_float(u << 16); }
__device__ __forceinline__ float hi16(unsigned u) { return __uint_as_float(u & 0xffff0000u); }
__device__ __forceinline__ unsigned pack2(float a, float b) { return (unsigned)f2bf(a) | ((unsigned)f2bf(b) << 16); }

typedef __attribute__((address_space(3))) s16x4 lds_s16x4;

__device__ __forceinline__ unsigned offB(unsigned row, unsigned ch) {
  return 256u * row + 16u * (ch ^ (((row & 3) << 2) | ((row >> 2) & 3)));
}
__device__ __forceinline__ bf16x8 ldB(const char* Bs, unsigned lane, unsigned c, unsigned ks) {
  const unsigned g = lane >> 4, q = (lane & 15) >> 2, p = lane & 3;
  unsigned a0 = offB(32 * ks + 8 * g + 0 + q, 2 * c + (p >> 1)) + 8 * (p & 1);
  unsigned a1 = offB(32 * ks + 8 * g + 4 + q, 2 * c + (p >> 1)) + 8 * (p & 1);
  s16x4 v0 = __builtin_amdgcn_ds_read_tr16_b64_v4i16((lds_s16x4*)(Bs + a0));
  s16x4 v1 = __builtin_amdgcn_ds_read_tr16_b64_v4i16((lds_s16x4*)(Bs + a1));
  bf16x8 r;
  r[0] = v0[0]; r[1] = v0[1]; r[2] = v0[2]; r[3] = v0[3];
  r[4] = v1[0]; r[5] = v1[1]; r[6] = v1[2]; r[7] = v1[3];
  return r;
}
__device__ __forceinline__ bf16x8 ldA512(const char* As, unsigned lane, unsigned row0, unsigned ks) {
  unsigned r = row0 + (lane & 15);
  unsigned ch = 4 * ks + (lane >> 4);
  return *reinterpret_cast<const bf16x8*>(As + r * 1024 + ((ch ^ (r & 15)) << 4));
}

template <int PASSES>
__device__ __forceinline__ void stage_ln(const unsigned short* __restrict__ src, char* As, unsigned wave,
                                         unsigned lane, unsigned rowbase_g) {
  const unsigned rr = lane >> 3, c = lane & 7;
  u32x4 v[PASSES][8];
#pragma unroll
  for (int p = 0; p < PASSES; ++p) {
    unsigned r = wave * (8 * PASSES) + p * 8 + rr;
    const u32x4* rp = reinterpret_cast<const u32x4*>(src + (size_t)(rowbase_g + r) * DM);
#pragma unroll
    for (int j = 0; j < 8; ++j) v[p][j] = rp[c + 8 * j];
  }
#pragma unroll
  for (int p = 0; p < PASSES; ++p) {
    unsigned r = wave * (8 * PASSES) + p * 8 + rr;
    float s = 0.f;
#pragma unroll
    for (int j = 0; j < 8; ++j)
#pragma unroll
      for (int e = 0; e < 4; ++e) s += lo16(v[p][j][e]) + hi16(v[p][j][e]);
    s += __shfl_xor(s, 1); s += __shfl_xor(s, 2); s += __shfl_xor(s, 4);
    float mean = s * (1.0f / DM);
    float q = 0.f;
#pragma unroll
    for (int j = 0; j < 8; ++j)
#pragma unroll
      for (int e = 0; e < 4; ++e) {
        float a = lo16(v[p][j][e]) - mean, b = hi16(v[p][j][e]) - mean;
        q += a * a + b * b;
      }
    q += __shfl_xor(q, 1); q += __shfl_xor(q, 2); q += __shfl_xor(q, 4);
    float rstd = 1.0f / sqrtf(q * (1.0f / DM) + 1e-5f);
#pragma unroll
    for (int j = 0; j < 8; ++j) {
      u32x4 o;
#pragma unroll
      for (int e = 0; e < 4; ++e)
        o[e] = pack2((lo16(v[p][j][e]) - mean) * rstd, (hi16(v[p][j][e]) - mean) * rstd);
      unsigned ch = c + 8 * j;
      *reinterpret_cast<u32x4*>(As + r * 1024 + ((ch ^ (r & 15)) << 4)) = o;
    }
  }
}

__global__ __launch_bounds__(256, 1) void k1_qkv(const unsigned short* __restrict__ x,
                                                 const unsigned short* __restrict__ wqkv,
                                                 unsigned short* __restrict__ q_out,
                                                 float* __restrict__ kvp, float* __restrict__ zp) {
  __shared__ __attribute__((aligned(16))) char sm[163840];
  char* As = sm;
  char* Bs = sm + 131072;
  const unsigned t = threadIdx.x, lane = t & 63, wave = t >> 6;
  const unsigned b = blockIdx.x, xcd = b & 7, idx = b >> 3;
  const unsigned rp = xcd * 2 + (idx & 1), hp = idx >> 1;
  const unsigned row0 = rp * 128;

  u32x4 breg[3];
  auto loadB = [&](int st) {
#pragma unroll
    for (int i = 0; i < 3; ++i) {
      unsigned id = t + 256 * i;
      unsigned kr = id / 12, ch = id % 12;
      unsigned part = ch >> 2, cc = ch & 3;
      const unsigned short* p = wqkv + (size_t)(st * 64 + kr) * 1536 + part * 512 + hp * 32 + cc * 8;
      breg[i] = *reinterpret_cast<const u32x4*>(p);
    }
  };
  auto writeB = [&](int buf) {
#pragma unroll
    for (int i = 0; i < 3; ++i) {
      unsigned id = t + 256 * i;
      unsigned kr = id / 12, ch = id % 12;
      *reinterpret_cast<u32x4*>(Bs + buf * 16384 + offB(kr, ch)) = breg[i];
    }
  };
  loadB(0);
  stage_ln<4>(x, As, wave, lane, row0);
  writeB(0);
  loadB(1);
  __syncthreads();

  f32x4 acc[2][6];
#pragma unroll
  for (int i = 0; i < 2; ++i)
#pragma unroll
    for (int j = 0; j < 6; ++j) acc[i][j] = f32x4{0.f, 0.f, 0.f, 0.f};

  for (int st = 0; st < 8; ++st) {
    const char* Bc = Bs + (st & 1) * 16384;
#pragma unroll
    for (int ks = 0; ks < 2; ++ks) {
      bf16x8 a0 = ldA512(As, lane, wave * 32, st * 2 + ks);
      bf16x8 a1 = ldA512(As, lane, wave * 32 + 16, st * 2 + ks);
#pragma unroll
      for (int c = 0; c < 6; ++c) {
        bf16x8 bb = ldB(Bc, lane, c, ks);
        acc[0][c] = __builtin_amdgcn_mfma_f32_16x16x32_bf16(a0, bb, acc[0][c], 0, 0, 0);
        acc[1][c] = __builtin_amdgcn_mfma_f32_16x16x32_bf16(a1, bb, acc[1][c], 0, 0, 0);
      }
    }
    if (st + 1 < 8) {
      writeB((st + 1) & 1);
      if (st + 2 < 8) loadB(st + 2);
    }
    __syncthreads();
  }
  char* T = sm;
  const unsigned PT = 208;
#pragma unroll
  for (int rb = 0; rb < 2; ++rb)
#pragma unroll
    for (int c = 0; c < 6; ++c)
#pragma unroll
      for (int j = 0; j < 4; ++j) {
        unsigned r = wave * 32 + rb * 16 + (lane >> 4) * 4 + j;
        float v = bf2f(f2bf(acc[rb][c][j]));
        float o;
        if (c < 4) {
          float ph = v > 0.f ? bf2f(f2bf(v + 1.0f)) : bf2f(f2bf(__expf(v)));
          o = ph + 1e-6f;
        } else {
          o = v;
        }
        *reinterpret_cast<unsigned short*>(T + r * PT + (c * 16 + (lane & 15)) * 2) = f2bf(o);
      }
  __syncthreads();
#pragma unroll
  for (int i = 0; i < 2; ++i) {
    unsigned id = t + 256 * i;
    unsigned r = id >> 2, cc = id & 3;
    u32x4 v = *reinterpret_cast<const u32x4*>(T + r * PT + cc * 16);
    *reinterpret_cast<u32x4*>(q_out + (size_t)(row0 + r) * DM + hp * 32 + cc * 8) = v;
  }
  if (wave < 2) {
    const unsigned g = lane >> 4, q = (lane & 15) >> 2, p = lane & 3;
    f32x4 kv = {0.f, 0.f, 0.f, 0.f}, zz = {0.f, 0.f, 0.f, 0.f};
    bf16x8 ones;
#pragma unroll
    for (int e = 0; e < 8; ++e) ones[e] = (short)0x3F80;
#pragma unroll
    for (int ks = 0; ks < 4; ++ks) {
      bf16x8 fa, fb;
#pragma unroll
      for (int tt = 0; tt < 2; ++tt) {
        unsigned r = 32 * ks + 8 * g + 4 * tt + q;
        s16x4 ka = __builtin_amdgcn_ds_read_tr16_b64_v4i16((lds_s16x4*)(T + r * PT + (32 + wave * 16 + 4 * p) * 2));
        s16x4 vb = __builtin_amdgcn_ds_read_tr16_b64_v4i16((lds_s16x4*)(T + r * PT + (64 + wave * 16 + 4 * p) * 2));
#pragma unroll
        for (int e = 0; e < 4; ++e) { fa[4 * tt + e] = ka[e]; fb[4 * tt + e] = vb[e]; }
      }
      kv = __builtin_amdgcn_mfma_f32_16x16x32_bf16(fa, fb, kv, 0, 0, 0);
      zz = __builtin_amdgcn_mfma_f32_16x16x32_bf16(fa, ones, zz, 0, 0, 0);
    }
    const unsigned head = hp * 2 + wave;
    float* dst = kvp + ((size_t)rp * 32 + head) * 256;
#pragma unroll
    for (int j = 0; j < 4; ++j) dst[((lane >> 4) * 4 + j) * 16 + (lane & 15)] = kv[j];
    if ((lane & 15) == 0) {
#pragma unroll
      for (int j = 0; j < 4; ++j) zp[((size_t)rp * 32 + head) * 16 + (lane >> 4) * 4 + j] = zz[j];
    }
  }
}

__global__ __launch_bounds__(256) void k1b_fold(const float* __restrict__ kvp, const float* __restrict__ zp,
                                                const unsigned short* __restrict__ wo,
                                                unsigned short* __restrict__ wp, float* __restrict__ z) {
  __shared__ float kv[256];
  const unsigned t = threadIdx.x, h = blockIdx.x >> 2, qd = blockIdx.x & 3;
  float s = 0.f;
#pragma unroll
  for (int p = 0; p < 16; ++p) s += kvp[((size_t)p * 32 + h) * 256 + t];
  kv[t] = s;
  if (qd == 0 && t < 16) {
    float zs = 0.f;
#pragma unroll
    for (int p = 0; p < 16; ++p) zs += zp[((size_t)p * 32 + h) * 16 + t];
    z[h * 16 + t] = zs;
  }
  __syncthreads();
  const unsigned d = t >> 4, cg = t & 15;
  const unsigned n0 = qd * 128 + cg * 8;
  float o[8];
#pragma unroll
  for (int i = 0; i < 8; ++i) o[i] = 0.f;
#pragma unroll
  for (int e = 0; e < 16; ++e) {
    u32x4 w = *reinterpret_cast<const u32x4*>(wo + (size_t)(h * 16 + e) * DM + n0);
    float kk = kv[d * 16 + e];
#pragma unroll
    for (int i = 0; i < 4; ++i) { o[2 * i] += kk * lo16(w[i]); o[2 * i + 1] += kk * hi16(w[i]); }
  }
  u32x4 r;
#pragma unroll
  for (int i = 0; i < 4; ++i) r[i] = pack2(o[2 * i], o[2 * i + 1]);
  *reinterpret_cast<u32x4*>(wp + (size_t)(h * 16 + d) * DM + n0) = r;
}

__global__ __launch_bounds__(256, 1) void k2_attn_out(const unsigned short* __restrict__ qv,
                                                      const float* __restrict__ z,
                                                      const unsigned short* __restrict__ wp,
                                                      const unsigned short* __restrict__ x,
                                                      unsigned short* __restrict__ h) {
  __shared__ __attribute__((aligned(16))) char sm[65536 + 2 * 16384];
  char* As = sm;
  char* Bs = sm + 65536;
  const unsigned t = threadIdx.x, lane = t & 63, wave = t >> 6;
  const unsigned b = blockIdx.x, xcd = b & 7, idx = b >> 3;
  const unsigned rp = xcd * 4 + (idx & 3), cp = idx >> 2;
  const unsigned row0 = rp * 64, col0 = cp * 64;

  u32x4 breg[2];
  auto loadB = [&](int st) {
#pragma unroll
    for (int i = 0; i < 2; ++i) {
      unsigned id = t + 256 * i;
      unsigned kr = id >> 3, ch = id & 7;
      breg[i] = *reinterpret_cast<const u32x4*>(wp + (size_t)(st * 64 + kr) * DM + col0 + ch * 8);
    }
  };
  auto writeB = [&](int buf) {
#pragma unroll
    for (int i = 0; i < 2; ++i) {
      unsigned id = t + 256 * i;
      unsigned kr = id >> 3, ch = id & 7;
      *reinterpret_cast<u32x4*>(Bs + buf * 16384 + offB(kr, ch)) = breg[i];
    }
  };
  loadB(0);
  {
    const unsigned rr = lane >> 3, c = lane & 7;
    u32x4 v[2][8];
#pragma unroll
    for (int p = 0; p < 2; ++p) {
      unsigned r = wave * 16 + p * 8 + rr;
      const u32x4* rpz = reinterpret_cast<const u32x4*>(qv + (size_t)(row0 + r) * DM);
#pragma unroll
      for (int j = 0; j < 8; ++j) v[p][j] = rpz[c + 8 * j];
    }
#pragma unroll
    for (int p = 0; p < 2; ++p) {
      unsigned r = wave * 16 + p * 8 + rr;
#pragma unroll
      for (int j = 0; j < 8; ++j) {
        unsigned ch = c + 8 * j;
        const f32x4* zp4 = reinterpret_cast<const f32x4*>(z + ch * 8);
        f32x4 z0 = zp4[0], z1 = zp4[1];
        float f[8];
#pragma unroll
        for (int e = 0; e < 4; ++e) { f[2 * e] = lo16(v[p][j][e]); f[2 * e + 1] = hi16(v[p][j][e]); }
        float dd = f[0] * z0[0] + f[1] * z0[1] + f[2] * z0[2] + f[3] * z0[3] + f[4] * z1[0] + f[5] * z1[1] +
                   f[6] * z1[2] + f[7] * z1[3];
        dd += __shfl_xor(dd, 1);
        float inv = 1.0f / (dd + 1e-6f);
        u32x4 o;
#pragma unroll
        for (int e = 0; e < 4; ++e) o[e] = pack2(f[2 * e] * inv, f[2 * e + 1] * inv);
        *reinterpret_cast<u32x4*>(As + r * 1024 + ((ch ^ (r & 15)) << 4)) = o;
      }
    }
  }
  writeB(0);
  loadB(1);
  __syncthreads();

  const unsigned wr = wave >> 1, wc = wave & 1;
  f32x4 acc[2][2];
#pragma unroll
  for (int i = 0; i < 2; ++i)
#pragma unroll
    for (int j = 0; j < 2; ++j) acc[i][j] = f32x4{0.f, 0.f, 0.f, 0.f};
  for (int st = 0; st < 8; ++st) {
    const char* Bc = Bs + (st & 1) * 16384;
#pragma unroll
    for (int ks = 0; ks < 2; ++ks) {
      bf16x8 a0 = ldA512(As, lane, wr * 32, st * 2 + ks);
      bf16x8 a1 = ldA512(As, lane, wr * 32 + 16, st * 2 + ks);
#pragma unroll
      for (int c = 0; c < 2; ++c) {
        bf16x8 bb = ldB(Bc, lane, wc * 2 + c, ks);
        acc[0][c] = __builtin_amdgcn_mfma_f32_16x16x32_bf16(a0, bb, acc[0][c], 0, 0, 0);
        acc[1][c] = __builtin_amdgcn_mfma_f32_16x16x32_bf16(a1, bb, acc[1][c], 0, 0, 0);
      }
    }
    if (st + 1 < 8) {
      writeB((st + 1) & 1);
      if (st + 2 < 8) loadB(st + 2);
    }
    __syncthreads();
  }
  float* T = reinterpret_cast<float*>(sm);
#pragma unroll
  for (int rb = 0; rb < 2; ++rb)
#pragma unroll
    for (int c = 0; c < 2; ++c)
#pragma unroll
      for (int j = 0; j < 4; ++j)
        T[(wr * 32 + rb * 16 + (lane >> 4) * 4 + j) * 68 + wc * 32 + c * 16 + (lane & 15)] = acc[rb][c][j];
  __syncthreads();
#pragma unroll
  for (int i = 0; i < 2; ++i) {
    unsigned id = t + 256 * i;
    unsigned r = id >> 3, cc = id & 7;
    size_t g = (size_t)(row0 + r) * DM + col0 + cc * 8;
    u32x4 xv = *reinterpret_cast<const u32x4*>(x + g);
    const float* tp = T + r * 68 + cc * 8;
    u32x4 o;
#pragma unroll
    for (int e = 0; e < 4; ++e) {
      float a = bf2f(f2bf(tp[2 * e])) + lo16(xv[e]);
      float c2 = bf2f(f2bf(tp[2 * e + 1])) + hi16(xv[e]);
      o[e] = pack2(a, c2);
    }
    *reinterpret_cast<u32x4*>(h + g) = o;
  }
}

__global__ __launch_bounds__(256, 1) void k3_ffn1(const unsigned short* __restrict__ h,
                                                  const unsigned short* __restrict__ w1,
                                                  unsigned short* __restrict__ h1) {
  __shared__ __attribute__((aligned(16))) char sm[163840];
  char* As = sm;
  char* Bs = sm + 131072;
  const unsigned t = threadIdx.x, lane = t & 63, wave = t >> 6;
  const unsigned b = blockIdx.x, xcd = b & 7, idx = b >> 3;
  const unsigned rp = xcd * 2 + (idx & 1), cp = idx >> 1;
  const unsigned row0 = rp * 128, col0 = cp * 112;

  u32x4 breg[4];
  auto loadB = [&](int st) {
#pragma unroll
    for (int i = 0; i < 4; ++i) {
      unsigned id = t + 256 * i;
      if (id < 896) {
        unsigned kr = id / 14, ch = id % 14;
        breg[i] = *reinterpret_cast<const u32x4*>(w1 + (size_t)(st * 64 + kr) * FF + col0 + ch * 8);
      }
    }
  };
  auto writeB = [&](int buf) {
#pragma unroll
    for (int i = 0; i < 4; ++i) {
      unsigned id = t + 256 * i;
      if (id < 896) {
        unsigned kr = id / 14, ch = id % 14;
        *reinterpret_cast<u32x4*>(Bs + buf * 16384 + offB(kr, ch)) = breg[i];
      }
    }
  };
  loadB(0);
  stage_ln<4>(h, As, wave, lane, row0);
  writeB(0);
  loadB(1);
  __syncthreads();

  f32x4 acc[2][7];
#pragma unroll
  for (int i = 0; i < 2; ++i)
#pragma unroll
    for (int j = 0; j < 7; ++j) acc[i][j] = f32x4{0.f, 0.f, 0.f, 0.f};
  for (int st = 0; st < 8; ++st) {
    const char* Bc = Bs + (st & 1) * 16384;
#pragma unroll
    for (int ks = 0; ks < 2; ++ks) {
      bf16x8 a0 = ldA512(As, lane, wave * 32, st * 2 + ks);
      bf16x8 a1 = ldA512(As, lane, wave * 32 + 16, st * 2 + ks);
#pragma unroll
      for (int c = 0; c < 7; ++c) {
        bf16x8 bb = ldB(Bc, lane, c, ks);
        acc[0][c] = __builtin_amdgcn_mfma_f32_16x16x32_bf16(a0, bb, acc[0][c], 0, 0, 0);
        acc[1][c] = __builtin_amdgcn_mfma_f32_16x16x32_bf16(a1, bb, acc[1][c], 0, 0, 0);
      }
    }
    if (st + 1 < 8) {
      writeB((st + 1) & 1);
      if (st + 2 < 8) loadB(st + 2);
    }
    __syncthreads();
  }
  char* T = sm;
  const unsigned PT = 240;
#pragma unroll
  for (int rb = 0; rb < 2; ++rb)
#pragma unroll
    for (int c = 0; c < 7; ++c)
#pragma unroll
      for (int j = 0; j < 4; ++j) {
        unsigned r = wave * 32 + rb * 16 + (lane >> 4) * 4 + j;
        float v = bf2f(f2bf(acc[rb][c][j]));
        float sg = v / (1.0f + __expf(-v));
        *reinterpret_cast<unsigned short*>(T + r * PT + (c * 16 + (lane & 15)) * 2) = f2bf(sg);
      }
  __syncthreads();
#pragma unroll
  for (int i = 0; i < 7; ++i) {
    unsigned id = t + 256 * i;
    unsigned r = id / 14, cc = id % 14;
    u32x4 v = *reinterpret_cast<const u32x4*>(T + r * PT + cc * 16);
    *reinterpret_cast<u32x4*>(h1 + (size_t)(row0 + r) * FF + col0 + cc * 8) = v;
  }
}

__device__ __forceinline__ unsigned offA64(unsigned r, unsigned ch) { return r * 128 + ((ch ^ ((r >> 1) & 7)) << 4); }

__global__ __launch_bounds__(256, 1) void k4_ffn2(const unsigned short* __restrict__ h1,
                                                  const unsigned short* __restrict__ w2,
                                                  const unsigned short* __restrict__ h,
                                                  unsigned short* __restrict__ out) {
  __shared__ __attribute__((aligned(16))) char sm[2 * (8192 + 16384)];
  const unsigned t = threadIdx.x, lane = t & 63, wave = t >> 6;
  const unsigned b = blockIdx.x, xcd = b & 7, idx = b >> 3;
  const unsigned rp = xcd * 4 + (idx & 3), cp = idx >> 2;
  const unsigned row0 = rp * 64, col0 = cp * 64;
  constexpr int NST = FF / 64;

  u32x4 areg[2], breg[2];
  auto load = [&](int st) {
#pragma unroll
    for (int i = 0; i < 2; ++i) {
      unsigned id = t + 256 * i;
      unsigned r = id >> 3, ch = id & 7;
      areg[i] = *reinterpret_cast<const u32x4*>(h1 + (size_t)(row0 + r) * FF + st * 64 + ch * 8);
      breg[i] = *reinterpret_cast<const u32x4*>(w2 + (size_t)(st * 64 + r) * DM + col0 + ch * 8);
    }
  };
  auto write = [&](int buf) {
    char* A = sm + buf * 24576;
    char* B = A + 8192;
#pragma unroll
    for (int i = 0; i < 2; ++i) {
      unsigned id = t + 256 * i;
      unsigned r = id >> 3, ch = id & 7;
      *reinterpret_cast<u32x4*>(A + offA64(r, ch)) = areg[i];
      *reinterpret_cast<u32x4*>(B + offB(r, ch)) = breg[i];
    }
  };
  load(0);
  write(0);
  load(1);
  __syncthreads();
  const unsigned wr = wave >> 1, wc = wave & 1;
  f32x4 acc[2][2];
#pragma unroll
  for (int i = 0; i < 2; ++i)
#pragma unroll
    for (int j = 0; j < 2; ++j) acc[i][j] = f32x4{0.f, 0.f, 0.f, 0.f};
  for (int st = 0; st < NST; ++st) {
    const char* A = sm + (st & 1) * 24576;
    const char* Bc = A + 8192;
#pragma unroll
    for (int ks = 0; ks < 2; ++ks) {
      unsigned r0 = wr * 32 + (lane & 15), r1 = r0 + 16, ch = 4 * ks + (lane >> 4);
      bf16x8 a0 = *reinterpret_cast<const bf16x8*>(A + offA64(r0, ch));
      bf16x8 a1 = *reinterpret_cast<const bf16x8*>(A + offA64(r1, ch));
#pragma unroll
      for (int c = 0; c < 2; ++c) {
        bf16x8 bb = ldB(Bc, lane, wc * 2 + c, ks);
        acc[0][c] = __builtin_amdgcn_mfma_f32_16x16x32_bf16(a0, bb, acc[0][c], 0, 0, 0);
        acc[1][c] = __builtin_amdgcn_mfma_f32_16x16x32_bf16(a1, bb, acc[1][c], 0, 0, 0);
      }
    }
    if (st + 1 < NST) {
      write((st + 1) & 1);
      if (st + 2 < NST) load(st + 2);
    }
    __syncthreads();
  }
  float* T = reinterpret_cast<float*>(sm);
#pragma unroll
  for (int rb = 0; rb < 2; ++rb)
#pragma unroll
    for (int c = 0; c < 2; ++c)
#pragma unroll
      for (int j = 0; j < 4; ++j)
        T[(wr * 32 + rb * 16 + (lane >> 4) * 4 + j) * 68 + wc * 32 + c * 16 + (lane & 15)] = acc[rb][c][j];
  __syncthreads();
#pragma unroll
  for (int i = 0; i < 2; ++i) {
    unsigned id = t + 256 * i;
    unsigned r = id >> 3, cc = id & 7;
    size_t g = (size_t)(row0 + r) * DM + col0 + cc * 8;
    u32x4 xv = *reinterpret_cast<const u32x4*>(h + g);
    const float* tp = T + r * 68 + cc * 8;
    u32x4 o;
#pragma unroll
    for (int e = 0; e < 4; ++e) {
      float a = bf2f(f2bf(tp[2 * e])) + lo16(xv[e]);
      float c2 = bf2f(f2bf(tp[2 * e + 1])) + hi16(xv[e]);
      o[e] = pack2(a, c2);
    }
    *reinterpret_cast<u32x4*>(out + g) = o;
  }
}

extern "C" void kernel_launch(void* const* d_in, const int* in_sizes, int n_in, void* d_out, int out_size,
                              void* d_ws, size_t ws_size, hipStream_t stream) {
  const unsigned short* x = (const unsigned short*)d_in[0];
  const unsigned short* wqkv = (const unsigned short*)d_in[1];
  const unsigned short* wo = (const unsigned short*)d_in[2];
  const unsigned short* w1 = (const unsigned short*)d_in[4];
  const unsigned short* w2 = (const unsigned short*)d_in[5];
  unsigned short* out = (unsigned short*)d_out;

  char* ws = (char*)d_ws;
  unsigned short* q = (unsigned short*)(ws);
  unsigned short* hbuf = (unsigned short*)(ws + (2u << 20));
  unsigned short* h1 = (unsigned short*)(ws + (4u << 20));
  float* kvp = (float*)(ws + (12u << 20));
  float* zp = (float*)(ws + (13u << 20));
  unsigned short* wp = (unsigned short*)(ws + (14u << 20));
  float* z = (float*)(ws + (15u << 20));

  k1_qkv<<<256, 256, 0, stream>>>(x, wqkv, q, kvp, zp);
  k1b_fold<<<128, 256, 0, stream>>>(kvp, zp, wo, wp, z);
  k2_attn_out<<<256, 256, 0, stream>>>(q, z, wp, x, hbuf);
  k3_ffn1<<<256, 256, 0, stream>>>(hbuf, w1, h1);
  k4_ffn2<<<256, 256, 0, stream>>>(h1, w2, hbuf, out);
}
```

```cpp
#include <hip/hip_runtime.h>
#include <hip/hip_bf16.h>

using bf16x8 = __attribute__((ext_vector_type(8))) short;
using s16x4  = __attribute__((ext_vector_type(4))) short;
using f32x4  = __attribute__((ext_vector_type(4))) float;
using u32x4  = __attribute__((ext_vector_type(4))) unsigned;
typedef __bf16 bf2 __attribute__((ext_vector_type(2)));

#define STAMP(kid,i) do{}while(0)

#define DM 512
#define FF 1792

__device__ __forceinline__ float lo16(unsigned u) { return __uint_as_float(u << 16); }
__device__ __forceinline__ float hi16(unsigned u) { return __uint_as_float(u & 0xffff0000u); }
__device__ __forceinline__ unsigned pack2(float a, float b) {
  bf2 v = {(__bf16)a, (__bf16)b};
  return *reinterpret_cast<unsigned*>(&v);
}
__device__ __forceinline__ float rbf(float a) {
  return lo16(pack2(a, 0.f));
}
__device__ __forceinline__ unsigned short bfbits(float a) { return (unsigned short)(pack2(a, 0.f) & 0xffffu); }

typedef __attribute__((address_space(3))) s16x4 lds_s16x4;

#define WAITV(n) asm volatile("s_waitcnt vmcnt(" #n ")" ::: "memory")
#define BARRIER() __builtin_amdgcn_s_barrier()
#define GLDS(g, l) __builtin_amdgcn_global_load_lds((const unsigned*)(g), (unsigned*)(l), 16, 0, 0)

__device__ __forceinline__ unsigned swzB(unsigned k) { return ((k & 3) << 2) | ((k >> 2) & 3); }
__device__ __forceinline__ unsigned offB(unsigned k, unsigned ch) { return 256u * k + 16u * (ch ^ swzB(k)); }
__device__ __forceinline__ unsigned swzB64(unsigned k) { return (((k >> 1) & 1) | (((k >> 3) & 1) << 1)) << 1; }
__device__ __forceinline__ unsigned offB64(unsigned k, unsigned ch) { return 128u * k + 16u * (ch ^ swzB64(k)); }
__device__ __forceinline__ unsigned swzA64(unsigned r) { return (r >> 1) & 7; }
__device__ __forceinline__ unsigned offA64(unsigned r, unsigned ch) { return r * 128 + ((ch ^ swzA64(r)) << 4); }

__device__ __forceinline__ bf16x8 join(s16x4 a, s16x4 b) {
  bf16x8 r;
  r[0] = a[0]; r[1] = a[1]; r[2] = a[2]; r[3] = a[3];
  r[4] = b[0]; r[5] = b[1]; r[6] = b[2]; r[7] = b[3];
  return r;
}
__device__ __forceinline__ bf16x8 ldB(const char* Bs, unsigned lane, unsigned c, unsigned ks) {
  const unsigned g = lane >> 4, q = (lane & 15) >> 2, p = lane & 3;
  unsigned a0 = offB(32 * ks + 8 * g + q, 2 * c + (p >> 1)) + 8 * (p & 1);
  unsigned a1 = offB(32 * ks + 8 * g + 4 + q, 2 * c + (p >> 1)) + 8 * (p & 1);
  return join(__builtin_amdgcn_ds_read_tr16_b64_v4i16((lds_s16x4*)(Bs + a0)),
              __builtin_amdgcn_ds_read_tr16_b64_v4i16((lds_s16x4*)(Bs + a1)));
}
__device__ __forceinline__ bf16x8 ldB64(const char* Bs, unsigned lane, unsigned c, unsigned ks) {
  const unsigned g = lane >> 4, q = (lane & 15) >> 2, p = lane & 3;
  unsigned a0 = offB64(32 * ks + 8 * g + q, 2 * c + (p >> 1)) + 8 * (p & 1);
  unsigned a1 = offB64(32 * ks + 8 * g + 4 + q, 2 * c + (p >> 1)) + 8 * (p & 1);
  return join(__builtin_amdgcn_ds_read_tr16_b64_v4i16((lds_s16x4*)(Bs + a0)),
              __builtin_amdgcn_ds_read_tr16_b64_v4i16((lds_s16x4*)(Bs + a1)));
}
__device__ __forceinline__ float dot2(unsigned a, unsigned b, float c) {
  return __builtin_amdgcn_fdot2_f32_bf16(*reinterpret_cast<bf2*>(&a), *reinterpret_cast<bf2*>(&b), c, false);
}
#define ONES2 0x3F803F80u

template <int MODE>
__device__ __forceinline__ void big_gemm(const unsigned short* __restrict__ A, const unsigned short* __restrict__ W,
                                         char* sm, unsigned row0, unsigned colp, f32x4 (&acc)[2][4],
                                         float (&rs)[2], float (&rq)[2], float (&cs)[4]) {
  constexpr int NCH = MODE == 0 ? 12 : 14;
  constexpr int LDW = MODE == 0 ? 1536 : FF;
  const unsigned t = threadIdx.x, lane = t & 63, wave = t >> 6;
  const unsigned wr = wave >> 1, wc = wave & 1;
  const int c0 = wc * 4, NC = MODE == 0 ? 3 : (wc ? 3 : 4);
  const int cbase = MODE == 0 ? wc * 3 : c0;

  const unsigned short* asrc[2];
  const unsigned short* bsrc[2];
#pragma unroll
  for (int i = 0; i < 2; ++i) {
    unsigned j = i * 8 + wave;
    unsigned r = j * 8 + (lane >> 3), chp = lane & 7;
    asrc[i] = A + (size_t)(row0 + r) * DM + ((chp ^ swzA64(r)) << 3);
    unsigned kr = j * 4 + (lane >> 4), ch = (lane & 15) ^ swzB(kr);
    if (ch >= (unsigned)NCH) ch = 0;
    unsigned col = MODE == 0 ? ((ch >> 2) * 512 + colp * 32 + (ch & 3) * 8) : (colp * 112 + ch * 8);
    bsrc[i] = W + (size_t)kr * LDW + col;
  }
  auto issue = [&](int st) {
    char* buf = sm + (st & 3) * 32768;
#pragma unroll
    for (int i = 0; i < 2; ++i) {
      GLDS(asrc[i] + st * 64, buf + (i * 8 + wave) * 1024);
      GLDS(bsrc[i] + (size_t)st * 64 * LDW, buf + 16384 + (i * 8 + wave) * 1024);
    }
  };
  issue(0); issue(1); issue(2);
#pragma unroll
  for (int i = 0; i < 2; ++i) { rs[i] = 0.f; rq[i] = 0.f; }
#pragma unroll
  for (int c = 0; c < 4; ++c) { cs[c] = 0.f; acc[0][c] = f32x4{0.f,0.f,0.f,0.f}; acc[1][c] = f32x4{0.f,0.f,0.f,0.f}; }

#pragma unroll 1
  for (int st = 0; st < 8; ++st) {
    if (st < 6) WAITV(8); else if (st == 6) WAITV(4); else WAITV(0);
    BARRIER();
    if (st + 3 < 8) issue(st + 3);
    const char* Ab = sm + (st & 3) * 32768;
    const char* Bb = Ab + 16384;
#pragma unroll
    for (int ks = 0; ks < 2; ++ks) {
      unsigned ch = 4 * ks + (lane >> 4);
      unsigned r0 = wr * 32 + (lane & 15);
      bf16x8 a0 = *reinterpret_cast<const bf16x8*>(Ab + offA64(r0, ch));
      bf16x8 a1 = *reinterpret_cast<const bf16x8*>(Ab + offA64(r0 + 16, ch));
      {
        u32x4 u0 = *reinterpret_cast<u32x4*>(&a0), u1 = *reinterpret_cast<u32x4*>(&a1);
#pragma unroll
        for (int e = 0; e < 4; ++e) {
          rs[0] = dot2(u0[e], ONES2, rs[0]); rq[0] = dot2(u0[e], u0[e], rq[0]);
          rs[1] = dot2(u1[e], ONES2, rs[1]); rq[1] = dot2(u1[e], u1[e], rq[1]);
        }
      }
#pragma unroll
      for (int c = 0; c < 4; ++c) {
        if (c < NC) {
          bf16x8 bb = ldB(Bb, lane, cbase + c, ks);
          u32x4 ub = *reinterpret_cast<u32x4*>(&bb);
#pragma unroll
          for (int e = 0; e < 4; ++e) cs[c] = dot2(ub[e], ONES2, cs[c]);
          acc[0][c] = __builtin_amdgcn_mfma_f32_16x16x32_bf16(a0, bb, acc[0][c], 0, 0, 0);
          acc[1][c] = __builtin_amdgcn_mfma_f32_16x16x32_bf16(a1, bb, acc[1][c], 0, 0, 0);
        }
      }
    }
  }
#pragma unroll
  for (int i = 0; i < 2; ++i) {
    rs[i] += __shfl_xor(rs[i], 16); rs[i] += __shfl_xor(rs[i], 32);
    rq[i] += __shfl_xor(rq[i], 16); rq[i] += __shfl_xor(rq[i], 32);
  }
#pragma unroll
  for (int c = 0; c < 4; ++c) { cs[c] += __shfl_xor(cs[c], 16); cs[c] += __shfl_xor(cs[c], 32); }
  BARRIER();
}

__device__ __forceinline__ void row_stats(float s, float q, unsigned lane, float (&mean)[4], float (&rstd)[4]) {
#pragma unroll
  for (int j = 0; j < 4; ++j) {
    unsigned src = (lane >> 4) * 4 + j;
    float ss = __shfl(s, src), qq = __shfl(q, src);
    float m = ss * (1.0f / DM);
    float var = qq * (1.0f / DM) - m * m;
    var = var < 0.f ? 0.f : var;
    mean[j] = m;
    rstd[j] = 1.0f / sqrtf(var + 1e-5f);
  }
}

__global__ __launch_bounds__(512, 1) void k1_qkv(const unsigned short* __restrict__ x,
                                                 const unsigned short* __restrict__ wqkv,
                                                 unsigned short* __restrict__ q_out,
                                                 float* __restrict__ kvp, float* __restrict__ zp) {
  __shared__ __attribute__((aligned(16))) char sm[131072];
  const unsigned t = threadIdx.x, lane = t & 63, wave = t >> 6;
  const unsigned b = blockIdx.x, xcd = b & 7, idx = b >> 3;
  const unsigned rp = xcd * 2 + (idx & 1), hp = idx >> 1;
  const unsigned row0 = rp * 128;
  STAMP(0,0);
  f32x4 acc[2][4]; float rs[2], rq[2], cs[4];
  big_gemm<0>(x, wqkv, sm, row0, hp, acc, rs, rq, cs);
  STAMP(0,3);
  const unsigned wr = wave >> 1, wc = wave & 1;
  char* T = sm;
  const unsigned PT = 208;
#pragma unroll
  for (int rb = 0; rb < 2; ++rb) {
    float mean[4], rstd[4];
    row_stats(rs[rb], rq[rb], lane, mean, rstd);
#pragma unroll
    for (int c = 0; c < 3; ++c) {
      const int cg = wc * 3 + c;
#pragma unroll
      for (int j = 0; j < 4; ++j) {
        unsigned r = wr * 32 + rb * 16 + (lane >> 4) * 4 + j;
        float v = rbf(rstd[j] * (acc[rb][c][j] - mean[j] * cs[c]));
        float o = v;
        if (cg < 4) o = rbf(v > 0.f ? v + 1.0f : __expf(v)) + 1e-6f;
        *reinterpret_cast<unsigned short*>(T + r * PT + (cg * 16 + (lane & 15)) * 2) = bfbits(o);
      }
    }
  }
  __syncthreads();
  {
    unsigned r = t >> 2, cc = t & 3;
    u32x4 v = *reinterpret_cast<const u32x4*>(T + r * PT + cc * 16);
    *reinterpret_cast<u32x4*>(q_out + (size_t)(row0 + r) * DM + hp * 32 + cc * 8) = v;
  }
  if (wave < 2) {
    const unsigned g = lane >> 4, q = (lane & 15) >> 2, p = lane & 3;
    f32x4 kv = {0.f, 0.f, 0.f, 0.f}, zz = {0.f, 0.f, 0.f, 0.f};
    bf16x8 ones;
#pragma unroll
    for (int e = 0; e < 8; ++e) ones[e] = (short)0x3F80;
#pragma unroll
    for (int ks = 0; ks < 4; ++ks) {
      bf16x8 fa, fb;
#pragma unroll
      for (int tt = 0; tt < 2; ++tt) {
        unsigned r = 32 * ks + 8 * g + 4 * tt + q;
        s16x4 ka = __builtin_amdgcn_ds_read_tr16_b64_v4i16((lds_s16x4*)(T + r * PT + (32 + wave * 16 + 4 * p) * 2));
        s16x4 vb = __builtin_amdgcn_ds_read_tr16_b64_v4i16((lds_s16x4*)(T + r * PT + (64 + wave * 16 + 4 * p) * 2));
#pragma unroll
        for (int e = 0; e < 4; ++e) { fa[4 * tt + e] = ka[e]; fb[4 * tt + e] = vb[e]; }
      }
      kv = __builtin_amdgcn_mfma_f32_16x16x32_bf16(fa, fb, kv, 0, 0, 0);
      zz = __builtin_amdgcn_mfma_f32_16x16x32_bf16(fa, ones, zz, 0, 0, 0);
    }
    const unsigned head = hp * 2 + wave;
    float* dst = kvp + ((size_t)rp * 32 + head) * 256;
#pragma unroll
    for (int j = 0; j < 4; ++j) dst[((lane >> 4) * 4 + j) * 16 + (lane & 15)] = kv[j];
    if ((lane & 15) == 0) {
#pragma unroll
      for (int j = 0; j < 4; ++j) zp[((size_t)rp * 32 + head) * 16 + (lane >> 4) * 4 + j] = zz[j];
    }
  }
  STAMP(0,4);
}

__global__ __launch_bounds__(256) void k1b_fold(const float* __restrict__ kvp, const float* __restrict__ zp,
                                                const unsigned short* __restrict__ wo,
                                                unsigned short* __restrict__ wp, float* __restrict__ z) {
  __shared__ float kv[256];
  const unsigned t = threadIdx.x, h = blockIdx.x >> 2, qd = blockIdx.x & 3;
  const unsigned d = t >> 4, cg = t & 15;
  const unsigned n0 = qd * 128 + cg * 8;
  u32x4 w[16];
#pragma unroll
  for (int e = 0; e < 16; ++e) w[e] = *reinterpret_cast<const u32x4*>(wo + (size_t)(h * 16 + e) * DM + n0);
  float s = 0.f;
#pragma unroll
  for (int p = 0; p < 16; ++p) s += kvp[((size_t)p * 32 + h) * 256 + t];
  kv[t] = s;
  if (qd == 0 && t < 16) {
    float zs = 0.f;
#pragma unroll
    for (int p = 0; p < 16; ++p) zs += zp[((size_t)p * 32 + h) * 16 + t];
    z[h * 16 + t] = zs;
  }
  __syncthreads();
  float o[8];
#pragma unroll
  for (int i = 0; i < 8; ++i) o[i] = 0.f;
#pragma unroll
  for (int e = 0; e < 16; ++e) {
    float kk = kv[d * 16 + e];
#pragma unroll
    for (int i = 0; i < 4; ++i) { o[2 * i] += kk * lo16(w[e][i]); o[2 * i + 1] += kk * hi16(w[e][i]); }
  }
  u32x4 r;
#pragma unroll
  for (int i = 0; i < 4; ++i) r[i] = pack2(o[2 * i], o[2 * i + 1]);
  *reinterpret_cast<u32x4*>(wp + (size_t)(h * 16 + d) * DM + n0) = r;
}

__device__ __forceinline__ void epi64(char* sm, f32x4 (&acc)[2][2], const unsigned short* __restrict__ res,
                                      unsigned short* __restrict__ dst, unsigned row0, unsigned col0) {
  const unsigned t = threadIdx.x, lane = t & 63, wave = t >> 6;
  const unsigned kh = wave >> 2, wr = (wave >> 1) & 1, wc = wave & 1;
  float* T = reinterpret_cast<float*>(sm) + kh * 64 * 68;
#pragma unroll
  for (int rb = 0; rb < 2; ++rb)
#pragma unroll
    for (int c = 0; c < 2; ++c)
#pragma unroll
      for (int j = 0; j < 4; ++j)
        T[(wr * 32 + rb * 16 + (lane >> 4) * 4 + j) * 68 + wc * 32 + c * 16 + (lane & 15)] = acc[rb][c][j];
  __syncthreads();
  {
    unsigned r = t >> 3, cc = t & 7;
    size_t g = (size_t)(row0 + r) * DM + col0 + cc * 8;
    u32x4 xv = *reinterpret_cast<const u32x4*>(res + g);
    const float* t0 = reinterpret_cast<float*>(sm) + r * 68 + cc * 8;
    const float* t1 = t0 + 64 * 68;
    u32x4 o;
#pragma unroll
    for (int e = 0; e < 4; ++e) {
      float a = rbf(t0[2 * e] + t1[2 * e]) + lo16(xv[e]);
      float c2 = rbf(t0[2 * e + 1] + t1[2 * e + 1]) + hi16(xv[e]);
      o[e] = pack2(a, c2);
    }
    *reinterpret_cast<u32x4*>(dst + g) = o;
  }
}

__global__ __launch_bounds__(512, 1) void k2_attn_out(const unsigned short* __restrict__ qv,
                                                      const float* __restrict__ z,
                                                      const unsigned short* __restrict__ wp,
                                                      const unsigned short* __restrict__ x,
                                                      unsigned short* __restrict__ h) {
  __shared__ __attribute__((aligned(16))) char sm[131072];
  char* As = sm;
  char* Bs = sm + 65536;
  const unsigned t = threadIdx.x, lane = t & 63, wave = t >> 6;
  const unsigned b = blockIdx.x, xcd = b & 7, idx = b >> 3;
  const unsigned rp = xcd * 4 + (idx & 3), cp = idx >> 2;
  const unsigned row0 = rp * 64, col0 = cp * 64;
#pragma unroll
  for (int i = 0; i < 8; ++i) {
    unsigned j = i * 8 + wave;
    unsigned kr = j * 8 + (lane >> 3), ch = (lane & 7) ^ swzB64(kr);
    GLDS(wp + (size_t)kr * DM + col0 + ch * 8, Bs + j * 1024);
  }
  {
    const unsigned rr = lane >> 3, c = lane & 7;
    unsigned r = wave * 8 + rr;
    const u32x4* rpz = reinterpret_cast<const u32x4*>(qv + (size_t)(row0 + r) * DM);
    u32x4 v[8];
#pragma unroll
    for (int j = 0; j < 8; ++j) v[j] = rpz[c + 8 * j];
#pragma unroll
    for (int j = 0; j < 8; ++j) {
      unsigned ch = c + 8 * j;
      const f32x4* zp4 = reinterpret_cast<const f32x4*>(z + ch * 8);
      f32x4 z0 = zp4[0], z1 = zp4[1];
      float f[8];
#pragma unroll
      for (int e = 0; e < 4; ++e) { f[2 * e] = lo16(v[j][e]); f[2 * e + 1] = hi16(v[j][e]); }
      float dd = f[0] * z0[0] + f[1] * z0[1] + f[2] * z0[2] + f[3] * z0[3] + f[4] * z1[0] + f[5] * z1[1] +
                 f[6] * z1[2] + f[7] * z1[3];
      dd += __shfl_xor(dd, 1);
      float inv = 1.0f / (dd + 1e-6f);
      u32x4 o;
#pragma unroll
      for (int e = 0; e < 4; ++e) o[e] = pack2(f[2 * e] * inv, f[2 * e + 1] * inv);
      *reinterpret_cast<u32x4*>(As + r * 1024 + ((ch ^ (r & 15)) << 4)) = o;
    }
  }
  WAITV(0);
  __syncthreads();
  const unsigned kh = wave >> 2, wr = (wave >> 1) & 1, wc = wave & 1;
  f32x4 acc[2][2];
#pragma unroll
  for (int i = 0; i < 2; ++i)
#pragma unroll
    for (int j = 0; j < 2; ++j) acc[i][j] = f32x4{0.f, 0.f, 0.f, 0.f};
#pragma unroll
  for (int kk = 0; kk < 8; ++kk) {
    unsigned ks = kk * 2 + kh;
    unsigned r0 = wr * 32 + (lane & 15), r1 = r0 + 16, ch = 4 * ks + (lane >> 4);
    bf16x8 a0 = *reinterpret_cast<const bf16x8*>(As + r0 * 1024 + ((ch ^ (r0 & 15)) << 4));
    bf16x8 a1 = *reinterpret_cast<const bf16x8*>(As + r1 * 1024 + ((ch ^ (r1 & 15)) << 4));
#pragma unroll
    for (int c = 0; c < 2; ++c) {
      bf16x8 bb = ldB64(Bs, lane, wc * 2 + c, ks);
      acc[0][c] = __builtin_amdgcn_mfma_f32_16x16x32_bf16(a0, bb, acc[0][c], 0, 0, 0);
      acc[1][c] = __builtin_amdgcn_mfma_f32_16x16x32_bf16(a1, bb, acc[1][c], 0, 0, 0);
    }
  }
  __syncthreads();
  epi64(sm, acc, x, h, row0, col0);
}

__global__ __launch_bounds__(512, 1) void k3_ffn1(const unsigned short* __restrict__ h,
                                                  const unsigned short* __restrict__ w1,
                                                  unsigned short* __restrict__ h1) {
  __shared__ __attribute__((aligned(16))) char sm[131072];
  const unsigned t = threadIdx.x, lane = t & 63, wave = t >> 6;
  const unsigned b = blockIdx.x, xcd = b & 7, idx = b >> 3;
  const unsigned rp = xcd * 2 + (idx & 1), cp = idx >> 1;
  const unsigned row0 = rp * 128, col0 = cp * 112;
  f32x4 acc[2][4]; float rs[2], rq[2], cs[4];
  big_gemm<1>(h, w1, sm, row0, cp, acc, rs, rq, cs);
  const unsigned wr = wave >> 1, wc = wave & 1;
  const int NC = wc ? 3 : 4;
  char* T = sm;
  const unsigned PT = 240;
#pragma unroll
  for (int rb = 0; rb < 2; ++rb) {
    float mean[4], rstd[4];
    row_stats(rs[rb], rq[rb], lane, mean, rstd);
#pragma unroll
    for (int c = 0; c < 4; ++c) {
      if (c < NC) {
#pragma unroll
        for (int j = 0; j < 4; ++j) {
          unsigned r = wr * 32 + rb * 16 + (lane >> 4) * 4 + j;
          float v = rbf(rstd[j] * (acc[rb][c][j] - mean[j] * cs[c]));
          float sg = v / (1.0f + __expf(-v));
          *reinterpret_cast<unsigned short*>(T + r * PT + ((wc * 4 + c) * 16 + (lane & 15)) * 2) = bfbits(sg);
        }
      }
    }
  }
  __syncthreads();
#pragma unroll
  for (int i = 0; i < 4; ++i) {
    unsigned id = t + 512 * i;
    if (id < 1792) {
      unsigned r = id / 14, cc = id % 14;
      u32x4 v = *reinterpret_cast<const u32x4*>(T + r * PT + cc * 16);
      *reinterpret_cast<u32x4*>(h1 + (size_t)(row0 + r) * FF + col0 + cc * 8) = v;
    }
  }
}

__global__ __launch_bounds__(512, 1) void k4_ffn2(const unsigned short* __restrict__ h1,
                                                  const unsigned short* __restrict__ w2,
                                                  const unsigned short* __restrict__ h,
                                                  unsigned short* __restrict__ out) {
  __shared__ __attribute__((aligned(16))) char sm[131072];
  const unsigned t = threadIdx.x, lane = t & 63, wave = t >> 6;
  const unsigned b = blockIdx.x, xcd = b & 7, idx = b >> 3;
  const unsigned rp = xcd * 4 + (idx & 3), cp = idx >> 2;
  const unsigned row0 = rp * 64, col0 = cp * 64;
  constexpr int NST = FF / 64;
  const unsigned r = wave * 8 + (lane >> 3);
  const unsigned short* asrc = h1 + (size_t)(row0 + r) * FF + (((lane & 7) ^ swzA64(r)) << 3);
  const unsigned short* bsrc = w2 + (size_t)r * DM + col0 + (((lane & 7) ^ swzB64(r)) << 3);
  auto issue = [&](int st) {
    char* buf = sm + (st & 7) * 16384;
    GLDS(asrc + st * 64, buf + wave * 1024);
    GLDS(bsrc + (size_t)st * 64 * DM, buf + 8192 + wave * 1024);
  };
#pragma unroll
  for (int s = 0; s < 7; ++s) issue(s);
  const unsigned kh = wave >> 2, wr = (wave >> 1) & 1, wc = wave & 1;
  f32x4 acc[2][2];
#pragma unroll
  for (int i = 0; i < 2; ++i)
#pragma unroll
    for (int j = 0; j < 2; ++j) acc[i][j] = f32x4{0.f, 0.f, 0.f, 0.f};
#pragma unroll 1
  for (int st = 0; st < NST; ++st) {
    if (st + 7 <= NST) WAITV(12); else WAITV(0);
    BARRIER();
    if (st + 7 < NST) issue(st + 7);
    const char* A = sm + (st & 7) * 16384;
    const char* Bc = A + 8192;
    unsigned r0 = wr * 32 + (lane & 15), r1 = r0 + 16, ch = 4 * kh + (lane >> 4);
    bf16x8 a0 = *reinterpret_cast<const bf16x8*>(A + offA64(r0, ch));
    bf16x8 a1 = *reinterpret_cast<const bf16x8*>(A + offA64(r1, ch));
#pragma unroll
    for (int c = 0; c < 2; ++c) {
      bf16x8 bb = ldB64(Bc, lane, wc * 2 + c, kh);
      acc[0][c] = __builtin_amdgcn_mfma_f32_16x16x32_bf16(a0, bb, acc[0][c], 0, 0, 0);
      acc[1][c] = __builtin_amdgcn_mfma_f32_16x16x32_bf16(a1, bb, acc[1][c], 0, 0, 0);
    }
  }
  __syncthreads();
  epi64(sm, acc, h, out, row0, col0);
}

extern "C" void kernel_launch(void* const* d_in, const int* in_sizes, int n_in, void* d_out, int out_size,
                              void* d_ws, size_t ws_size, hipStream_t stream) {
  const unsigned short* x = (const unsigned short*)d_in[0];
  const unsigned short* wqkv = (const unsigned short*)d_in[1];
  const unsigned short* wo = (const unsigned short*)d_in[2];
  const unsigned short* w1 = (const unsigned short*)d_in[4];
  const unsigned short* w2 = (const unsigned short*)d_in[5];
  unsigned short* out = (unsigned short*)d_out;

  char* ws = (char*)d_ws;
  unsigned short* q = (unsigned short*)(ws);
  unsigned short* hbuf = (unsigned short*)(ws + (2u << 20));
  unsigned short* h1 = (unsigned short*)(ws + (4u << 20));
  float* kvp = (float*)(ws + (12u << 20));
  float* zp = (float*)(ws + (13u << 20));
  unsigned short* wp = (unsigned short*)(ws + (14u << 20));
  float* z = (float*)(ws + (15u << 20));
  k1_qkv<<<256, 512, 0, stream>>>(x, wqkv, q, kvp, zp);
  k1b_fold<<<128, 256, 0, stream>>>(kvp, zp, wo, wp, z);
  k2_attn_out<<<256, 512, 0, stream>>>(q, z, wp, x, hbuf);
  k3_ffn1<<<256, 512, 0, stream>>>(hbuf, w1, h1);
  k4_ffn2<<<256, 512, 0, stream>>>(h1, w2, hbuf, out);
}
```

```cpp
#include <hip/hip_runtime.h>
#include <hip/hip_bf16.h>

using bf16x8 = __attribute__((ext_vector_type(8))) short;
using s16x4  = __attribute__((ext_vector_type(4))) short;
using f32x4  = __attribute__((ext_vector_type(4))) float;
using u32x4  = __attribute__((ext_vector_type(4))) unsigned;
typedef __bf16 bf2 __attribute__((ext_vector_type(2)));

#define STAMP(kid,i) do{}while(0)

#define DM 512
#define ABL 2
#define FF 1792

__device__ __forceinline__ float lo16(unsigned u) { return __uint_as_float(u << 16); }
__device__ __forceinline__ float hi16(unsigned u) { return __uint_as_float(u & 0xffff0000u); }
__device__ __forceinline__ unsigned pack2(float a, float b) {
  bf2 v = {(__bf16)a, (__bf16)b};
  return *reinterpret_cast<unsigned*>(&v);
}
__device__ __forceinline__ float rbf(float a) {
  return lo16(pack2(a, 0.f));
}
__device__ __forceinline__ unsigned short bfbits(float a) { return (unsigned short)(pack2(a, 0.f) & 0xffffu); }

typedef __attribute__((address_space(3))) s16x4 lds_s16x4;

#define WAITV(n) asm volatile("s_waitcnt vmcnt(" #n ")" ::: "memory")
#define BARRIER() __builtin_amdgcn_s_barrier()
#define GLDS(g, l) __builtin_amdgcn_global_load_lds((const unsigned*)(g), (unsigned*)(l), 16, 0, 0)

__device__ __forceinline__ unsigned swzB(unsigned k) { return ((k & 3) << 2) | ((k >> 2) & 3); }
__device__ __forceinline__ unsigned offB(unsigned k, unsigned ch) { return 256u * k + 16u * (ch ^ swzB(k)); }
__device__ __forceinline__ unsigned swzB64(unsigned k) { return (((k >> 1) & 1) | (((k >> 3) & 1) << 1)) << 1; }
__device__ __forceinline__ unsigned offB64(unsigned k, unsigned ch) { return 128u * k + 16u * (ch ^ swzB64(k)); }
__device__ __forceinline__ unsigned swzA64(unsigned r) { return (r >> 1) & 7; }
__device__ __forceinline__ unsigned offA64(unsigned r, unsigned ch) { return r * 128 + ((ch ^ swzA64(r)) << 4); }

__device__ __forceinline__ bf16x8 join(s16x4 a, s16x4 b) {
  bf16x8 r;
  r[0] = a[0]; r[1] = a[1]; r[2] = a[2]; r[3] = a[3];
  r[4] = b[0]; r[5] = b[1]; r[6] = b[2]; r[7] = b[3];
  return r;
}
__device__ __forceinline__ bf16x8 tr2(const char* Bs, unsigned a0, unsigned a1) {
  unsigned base = (unsigned)(size_t)(__attribute__((address_space(3))) const char*)Bs;
  s16x4 v0, v1;
  asm volatile("ds_read_b64_tr_b16 %0, %2\n\tds_read_b64_tr_b16 %1, %3\n\ts_waitcnt lgkmcnt(0)"
               : "=&v"(v0), "=&v"(v1) : "v"(base + a0), "v"(base + a1));
  return join(v0, v1);
}
__device__ __forceinline__ bf16x8 ldB(const char* Bs, unsigned lane, unsigned c, unsigned ks) {
  const unsigned g = lane >> 4, q = (lane & 15) >> 2, p = lane & 3;
  unsigned a0 = offB(32 * ks + 8 * g + q, 2 * c + (p >> 1)) + 8 * (p & 1);
  unsigned a1 = offB(32 * ks + 8 * g + 4 + q, 2 * c + (p >> 1)) + 8 * (p & 1);
  return tr2(Bs, a0, a1);
}
__device__ __forceinline__ bf16x8 ldB64(const char* Bs, unsigned lane, unsigned c, unsigned ks) {
  const unsigned g = lane >> 4, q = (lane & 15) >> 2, p = lane & 3;
  unsigned a0 = offB64(32 * ks + 8 * g + q, 2 * c + (p >> 1)) + 8 * (p & 1);
  unsigned a1 = offB64(32 * ks + 8 * g + 4 + q, 2 * c + (p >> 1)) + 8 * (p & 1);
  return tr2(Bs, a0, a1);
}
__device__ __forceinline__ float dot2(unsigned a, unsigned b, float c) {
  return __builtin_amdgcn_fdot2_f32_bf16(*reinterpret_cast<bf2*>(&a), *reinterpret_cast<bf2*>(&b), c, false);
}
#define ONES2 0x3F803F80u

template <int MODE>
__device__ __forceinline__ void big_gemm(const unsigned short* __restrict__ A, const unsigned short* __restrict__ W,
                                         char* sm, unsigned row0, unsigned colp, f32x4 (&acc)[2][4],
                                         float (&rs)[2], float (&rq)[2], float (&cs)[4]) {
  constexpr int NCH = MODE == 0 ? 12 : 14;
  constexpr int LDW = MODE == 0 ? 1536 : FF;
  const unsigned t = threadIdx.x, lane = t & 63, wave = t >> 6;
  const unsigned wr = wave >> 1, wc = wave & 1;
  const int c0 = wc * 4, NC = MODE == 0 ? 3 : (wc ? 3 : 4);
  const int cbase = MODE == 0 ? wc * 3 : c0;

  const unsigned short* asrc[2];
  const unsigned short* bsrc[2];
#pragma unroll
  for (int i = 0; i < 2; ++i) {
    unsigned j = i * 8 + wave;
    unsigned r = j * 8 + (lane >> 3), chp = lane & 7;
    asrc[i] = A + (size_t)(row0 + r) * DM + ((chp ^ swzA64(r)) << 3);
    unsigned kr = j * 4 + (lane >> 4), ch = (lane & 15) ^ swzB(kr);
    if (ch >= (unsigned)NCH) ch = 0;
    unsigned col = MODE == 0 ? ((ch >> 2) * 512 + colp * 32 + (ch & 3) * 8) : (colp * 112 + ch * 8);
    bsrc[i] = W + (size_t)kr * LDW + col;
  }
  auto issue = [&](int st) {
    char* buf = sm + (st & 3) * 32768;
#pragma unroll
    for (int i = 0; i < 2; ++i) {
      GLDS(asrc[i] + st * 64, buf + (i * 8 + wave) * 1024);
      GLDS(bsrc[i] + (size_t)st * 64 * LDW, buf + 16384 + (i * 8 + wave) * 1024);
    }
  };
  issue(0); issue(1); issue(2);
#pragma unroll
  for (int i = 0; i < 2; ++i) { rs[i] = 0.f; rq[i] = 0.f; }
#pragma unroll
  for (int c = 0; c < 4; ++c) { cs[c] = 0.f; acc[0][c] = f32x4{0.f,0.f,0.f,0.f}; acc[1][c] = f32x4{0.f,0.f,0.f,0.f}; }

#pragma unroll 1
  for (int st = 0; st < 8; ++st) {
    if (st < 6) WAITV(8); else if (st == 6) WAITV(4); else WAITV(0);
    BARRIER();
    if (st + 3 < 8) issue(st + 3);
    const char* Ab = sm + (st & 3) * 32768;
    const char* Bb = Ab + 16384;
#pragma unroll
    for (int ks = 0; ks < ABL; ++ks) {
      unsigned ch = 4 * ks + (lane >> 4);
      unsigned r0 = wr * 32 + (lane & 15);
      bf16x8 a0 = *reinterpret_cast<const bf16x8*>(Ab + offA64(r0, ch));
      bf16x8 a1 = *reinterpret_cast<const bf16x8*>(Ab + offA64(r0 + 16, ch));
      {
        u32x4 u0 = *reinterpret_cast<u32x4*>(&a0), u1 = *reinterpret_cast<u32x4*>(&a1);
#pragma unroll
        for (int e = 0; e < 4; ++e) {
          rs[0] = dot2(u0[e], ONES2, rs[0]); rq[0] = dot2(u0[e], u0[e], rq[0]);
          rs[1] = dot2(u1[e], ONES2, rs[1]); rq[1] = dot2(u1[e], u1[e], rq[1]);
        }
      }
      bf16x8 bq[4];
#pragma unroll
      for (int c = 0; c < 4; ++c) if (c < NC) bq[c] = ldB(Bb, lane, cbase + c, ks);
      asm volatile("s_waitcnt lgkmcnt(0)" ::: "memory"); __builtin_amdgcn_sched_barrier(0);
#pragma unroll
      for (int c = 0; c < 4; ++c) {
        if (c < NC) {
          bf16x8 bb = bq[c];
          u32x4 ub = *reinterpret_cast<u32x4*>(&bb);
#pragma unroll
          for (int e = 0; e < 4; ++e) cs[c] = dot2(ub[e], ONES2, cs[c]);
          acc[0][c] = __builtin_amdgcn_mfma_f32_16x16x32_bf16(a0, bb, acc[0][c], 0, 0, 0);
          acc[1][c] = __builtin_amdgcn_mfma_f32_16x16x32_bf16(a1, bb, acc[1][c], 0, 0, 0);
        }
      }
    }
  }
#pragma unroll
  for (int i = 0; i < 2; ++i) {
    rs[i] += __shfl_xor(rs[i], 16); rs[i] += __shfl_xor(rs[i], 32);
    rq[i] += __shfl_xor(rq[i], 16); rq[i] += __shfl_xor(rq[i], 32);
  }
#pragma unroll
  for (int c = 0; c < 4; ++c) { cs[c] += __shfl_xor(cs[c], 16); cs[c] += __shfl_xor(cs[c], 32); }
  BARRIER();
}

__device__ __forceinline__ void row_stats(float s, float q, unsigned lane, float (&mean)[4], float (&rstd)[4]) {
#pragma unroll
  for (int j = 0; j < 4; ++j) {
    unsigned src = (lane >> 4) * 4 + j;
    float ss = __shfl(s, src), qq = __shfl(q, src);
    float m = ss * (1.0f / DM);
    float var = qq * (1.0f / DM) - m * m;
    var = var < 0.f ? 0.f : var;
    mean[j] = m;
    rstd[j] = 1.0f / sqrtf(var + 1e-5f);
  }
}

__global__ __launch_bounds__(512, 1) void k1_qkv(const unsigned short* __restrict__ x,
                                                 const unsigned short* __restrict__ wqkv,
                                                 unsigned short* __restrict__ q_out,
                                                 float* __restrict__ kvp, float* __restrict__ zp) {
  __shared__ __attribute__((aligned(16))) char sm[131072];
  const unsigned t = threadIdx.x, lane = t & 63, wave = t >> 6;
  const unsigned b = blockIdx.x, xcd = b & 7, idx = b >> 3;
  const unsigned rp = xcd * 2 + (idx & 1), hp = idx >> 1;
  const unsigned row0 = rp * 128;
  STAMP(0,0);
  f32x4 acc[2][4]; float rs[2], rq[2], cs[4];
  big_gemm<0>(x, wqkv, sm, row0, hp, acc, rs, rq, cs);
  STAMP(0,3);
  const unsigned wr = wave >> 1, wc = wave & 1;
  char* T = sm;
  const unsigned PT = 208;
#pragma unroll
  for (int rb = 0; rb < 2; ++rb) {
    float mean[4], rstd[4];
    row_stats(rs[rb], rq[rb], lane, mean, rstd);
#pragma unroll
    for (int c = 0; c < 3; ++c) {
      const int cg = wc * 3 + c;
#pragma unroll
      for (int j = 0; j < 4; ++j) {
        unsigned r = wr * 32 + rb * 16 + (lane >> 4) * 4 + j;
        float v = rbf(rstd[j] * (acc[rb][c][j] - mean[j] * cs[c]));
        float o = v;
        if (cg < 4) o = rbf(v > 0.f ? v + 1.0f : __expf(v)) + 1e-6f;
        *reinterpret_cast<unsigned short*>(T + r * PT + (cg * 16 + (lane & 15)) * 2) = bfbits(o);
      }
    }
  }
  __syncthreads();
  {
    unsigned r = t >> 2, cc = t & 3;
    u32x4 v = *reinterpret_cast<const u32x4*>(T + r * PT + cc * 16);
    *reinterpret_cast<u32x4*>(q_out + (size_t)(row0 + r) * DM + hp * 32 + cc * 8) = v;
  }
  if (wave < 2) {
    const unsigned g = lane >> 4, q = (lane & 15) >> 2, p = lane & 3;
    f32x4 kv = {0.f, 0.f, 0.f, 0.f}, zz = {0.f, 0.f, 0.f, 0.f};
    bf16x8 ones;
#pragma unroll
    for (int e = 0; e < 8; ++e) ones[e] = (short)0x3F80;
#pragma unroll
    for (int ks = 0; ks < 4; ++ks) {
      bf16x8 fa, fb;
#pragma unroll
      for (int tt = 0; tt < 2; ++tt) {
        unsigned r = 32 * ks + 8 * g + 4 * tt + q;
        s16x4 ka = __builtin_amdgcn_ds_read_tr16_b64_v4i16((lds_s16x4*)(T + r * PT + (32 + wave * 16 + 4 * p) * 2));
        s16x4 vb = __builtin_amdgcn_ds_read_tr16_b64_v4i16((lds_s16x4*)(T + r * PT + (64 + wave * 16 + 4 * p) * 2));
#pragma unroll
        for (int e = 0; e < 4; ++e) { fa[4 * tt + e] = ka[e]; fb[4 * tt + e] = vb[e]; }
      }
      kv = __builtin_amdgcn_mfma_f32_16x16x32_bf16(fa, fb, kv, 0, 0, 0);
      zz = __builtin_amdgcn_mfma_f32_16x16x32_bf16(fa, ones, zz, 0, 0, 0);
    }
    const unsigned head = hp * 2 + wave;
    float* dst = kvp + ((size_t)rp * 32 + head) * 256;
#pragma unroll
    for (int j = 0; j < 4; ++j) dst[((lane >> 4) * 4 + j) * 16 + (lane & 15)] = kv[j];
    if ((lane & 15) == 0) {
#pragma unroll
      for (int j = 0; j < 4; ++j) zp[((size_t)rp * 32 + head) * 16 + (lane >> 4) * 4 + j] = zz[j];
    }
  }
  STAMP(0,4);
}

__global__ __launch_bounds__(256) void k1b_fold(const float* __restrict__ kvp, const float* __restrict__ zp,
                                                const unsigned short* __restrict__ wo,
                                                unsigned short* __restrict__ wp, float* __restrict__ z) {
  __shared__ float kv[256];
  const unsigned t = threadIdx.x, h = blockIdx.x >> 2, qd = blockIdx.x & 3;
  const unsigned d = t >> 4, cg = t & 15;
  const unsigned n0 = qd * 128 + cg * 8;
  u32x4 w[16];
#pragma unroll
  for (int e = 0; e < 16; ++e) w[e] = *reinterpret_cast<const u32x4*>(wo + (size_t)(h * 16 + e) * DM + n0);
  float s = 0.f;
#pragma unroll
  for (int p = 0; p < 16; ++p) s += kvp[((size_t)p * 32 + h) * 256 + t];
  kv[t] = s;
  if (qd == 0 && t < 16) {
    float zs = 0.f;
#pragma unroll
    for (int p = 0; p < 16; ++p) zs += zp[((size_t)p * 32 + h) * 16 + t];
    z[h * 16 + t] = zs;
  }
  __syncthreads();
  float o[8];
#pragma unroll
  for (int i = 0; i < 8; ++i) o[i] = 0.f;
#pragma unroll
  for (int e = 0; e < 16; ++e) {
    float kk = kv[d * 16 + e];
#pragma unroll
    for (int i = 0; i < 4; ++i) { o[2 * i] += kk * lo16(w[e][i]); o[2 * i + 1] += kk * hi16(w[e][i]); }
  }
  u32x4 r;
#pragma unroll
  for (int i = 0; i < 4; ++i) r[i] = pack2(o[2 * i], o[2 * i + 1]);
  *reinterpret_cast<u32x4*>(wp + (size_t)(h * 16 + d) * DM + n0) = r;
}

__device__ __forceinline__ void epi64(char* sm, f32x4 (&acc)[2][2], const unsigned short* __restrict__ res,
                                      unsigned short* __restrict__ dst, unsigned row0, unsigned col0) {
  const unsigned t = threadIdx.x, lane = t & 63, wave = t >> 6;
  const unsigned kh = wave >> 2, wr = (wave >> 1) & 1, wc = wave & 1;
  float* T = reinterpret_cast<float*>(sm) + kh * 64 * 68;
#pragma unroll
  for (int rb = 0; rb < 2; ++rb)
#pragma unroll
    for (int c = 0; c < 2; ++c)
#pragma unroll
      for (int j = 0; j < 4; ++j)
        T[(wr * 32 + rb * 16 + (lane >> 4) * 4 + j) * 68 + wc * 32 + c * 16 + (lane & 15)] = acc[rb][c][j];
  __syncthreads();
  {
    unsigned r = t >> 3, cc = t & 7;
    size_t g = (size_t)(row0 + r) * DM + col0 + cc * 8;
    u32x4 xv = *reinterpret_cast<const u32x4*>(res + g);
    const float* t0 = reinterpret_cast<float*>(sm) + r * 68 + cc * 8;
    const float* t1 = t0 + 64 * 68;
    u32x4 o;
#pragma unroll
    for (int e = 0; e < 4; ++e) {
      float a = rbf(t0[2 * e] + t1[2 * e]) + lo16(xv[e]);
      float c2 = rbf(t0[2 * e + 1] + t1[2 * e + 1]) + hi16(xv[e]);
      o[e] = pack2(a, c2);
    }
    *reinterpret_cast<u32x4*>(dst + g) = o;
  }
}

__global__ __launch_bounds__(512, 1) void k2_attn_out(const unsigned short* __restrict__ qv,
                                                      const float* __restrict__ z,
                                                      const unsigned short* __restrict__ wp,
                                                      const unsigned short* __restrict__ x,
                                                      unsigned short* __restrict__ h) {
  __shared__ __attribute__((aligned(16))) char sm[131072];
  char* As = sm;
  char* Bs = sm + 65536;
  const unsigned t = threadIdx.x, lane = t & 63, wave = t >> 6;
  const unsigned b = blockIdx.x, xcd = b & 7, idx = b >> 3;
  const unsigned rp = xcd * 4 + (idx & 3), cp = idx >> 2;
  const unsigned row0 = rp * 64, col0 = cp * 64;
#pragma unroll
  for (int i = 0; i < 8; ++i) {
    unsigned j = i * 8 + wave;
    unsigned kr = j * 8 + (lane >> 3), ch = (lane & 7) ^ swzB64(kr);
    GLDS(wp + (size_t)kr * DM + col0 + ch * 8, Bs + j * 1024);
  }
  {
    const unsigned rr = lane >> 3, c = lane & 7;
    unsigned r = wave * 8 + rr;
    const u32x4* rpz = reinterpret_cast<const u32x4*>(qv + (size_t)(row0 + r) * DM);
    u32x4 v[8];
#pragma unroll
    for (int j = 0; j < 8; ++j) v[j] = rpz[c + 8 * j];
#pragma unroll
    for (int j = 0; j < 8; ++j) {
      unsigned ch = c + 8 * j;
      const f32x4* zp4 = reinterpret_cast<const f32x4*>(z + ch * 8);
      f32x4 z0 = zp4[0], z1 = zp4[1];
      float f[8];
#pragma unroll
      for (int e = 0; e < 4; ++e) { f[2 * e] = lo16(v[j][e]); f[2 * e + 1] = hi16(v[j][e]); }
      float dd = f[0] * z0[0] + f[1] * z0[1] + f[2] * z0[2] + f[3] * z0[3] + f[4] * z1[0] + f[5] * z1[1] +
                 f[6] * z1[2] + f[7] * z1[3];
      dd += __shfl_xor(dd, 1);
      float inv = 1.0f / (dd + 1e-6f);
      u32x4 o;
#pragma unroll
      for (int e = 0; e < 4; ++e) o[e] = pack2(f[2 * e] * inv, f[2 * e + 1] * inv);
      *reinterpret_cast<u32x4*>(As + r * 1024 + ((ch ^ (r & 15)) << 4)) = o;
    }
  }
  WAITV(0);
  __syncthreads();
  const unsigned kh = wave >> 2, wr = (wave >> 1) & 1, wc = wave & 1;
  f32x4 acc[2][2];
#pragma unroll
  for (int i = 0; i < 2; ++i)
#pragma unroll
    for (int j = 0; j < 2; ++j) acc[i][j] = f32x4{0.f, 0.f, 0.f, 0.f};
#pragma unroll
  for (int kk = 0; kk < 8; ++kk) {
    unsigned ks = kk * 2 + kh;
    unsigned r0 = wr * 32 + (lane & 15), r1 = r0 + 16, ch = 4 * ks + (lane >> 4);
    bf16x8 a0 = *reinterpret_cast<const bf16x8*>(As + r0 * 1024 + ((ch ^ (r0 & 15)) << 4));
    bf16x8 a1 = *reinterpret_cast<const bf16x8*>(As + r1 * 1024 + ((ch ^ (r1 & 15)) << 4));
    bf16x8 bq[2];
    bq[0] = ldB64(Bs, lane, wc * 2, ks); bq[1] = ldB64(Bs, lane, wc * 2 + 1, ks);
    asm volatile("s_waitcnt lgkmcnt(0)" ::: "memory"); __builtin_amdgcn_sched_barrier(0);
#pragma unroll
    for (int c = 0; c < 2; ++c) {
      bf16x8 bb = bq[c];
      acc[0][c] = __builtin_amdgcn_mfma_f32_16x16x32_bf16(a0, bb, acc[0][c], 0, 0, 0);
      acc[1][c] = __builtin_amdgcn_mfma_f32_16x16x32_bf16(a1, bb, acc[1][c], 0, 0, 0);
    }
  }
  __syncthreads();
  epi64(sm, acc, x, h, row0, col0);
}

__global__ __launch_bounds__(512, 1) void k3_ffn1(const unsigned short* __restrict__ h,
                                                  const unsigned short* __restrict__ w1,
                                                  unsigned short* __restrict__ h1) {
  __shared__ __attribute__((aligned(16))) char sm[131072];
  const unsigned t = threadIdx.x, lane = t & 63, wave = t >> 6;
  const unsigned b = blockIdx.x, xcd = b & 7, idx = b >> 3;
  const unsigned rp = xcd * 2 + (idx & 1), cp = idx >> 1;
  const unsigned row0 = rp * 128, col0 = cp * 112;
  f32x4 acc[2][4]; float rs[2], rq[2], cs[4];
  big_gemm<1>(h, w1, sm, row0, cp, acc, rs, rq, cs);
  const unsigned wr = wave >> 1, wc = wave & 1;
  const int NC = wc ? 3 : 4;
  char* T = sm;
  const unsigned PT = 240;
#pragma unroll
  for (int rb = 0; rb < 2; ++rb) {
    float mean[4], rstd[4];
    row_stats(rs[rb], rq[rb], lane, mean, rstd);
#pragma unroll
    for (int c = 0; c < 4; ++c) {
      if (c < NC) {
#pragma unroll
        for (int j = 0; j < 4; ++j) {
          unsigned r = wr * 32 + rb * 16 + (lane >> 4) * 4 + j;
          float v = rbf(rstd[j] * (acc[rb][c][j] - mean[j] * cs[c]));
          float sg = v / (1.0f + __expf(-v));
          *reinterpret_cast<unsigned short*>(T + r * PT + ((wc * 4 + c) * 16 + (lane & 15)) * 2) = bfbits(sg);
        }
      }
    }
  }
  __syncthreads();
#pragma unroll
  for (int i = 0; i < 4; ++i) {
    unsigned id = t + 512 * i;
    if (id < 1792) {
      unsigned r = id / 14, cc = id % 14;
      u32x4 v = *reinterpret_cast<const u32x4*>(T + r * PT + cc * 16);
      *reinterpret_cast<u32x4*>(h1 + (size_t)(row0 + r) * FF + col0 + cc * 8) = v;
    }
  }
}

__global__ __launch_bounds__(512, 1) void k4_ffn2(const unsigned short* __restrict__ h1,
                                                  const unsigned short* __restrict__ w2,
                                                  const unsigned short* __restrict__ h,
                                                  unsigned short* __restrict__ out) {
  __shared__ __attribute__((aligned(16))) char sm[131072];
  const unsigned t = threadIdx.x, lane = t & 63, wave = t >> 6;
  const unsigned b = blockIdx.x, xcd = b & 7, idx = b >> 3;
  const unsigned rp = xcd * 4 + (idx & 3), cp = idx >> 2;
  const unsigned row0 = rp * 64, col0 = cp * 64;
  constexpr int NST = FF / 64;
  const unsigned r = wave * 8 + (lane >> 3);
  const unsigned short* asrc = h1 + (size_t)(row0 + r) * FF + (((lane & 7) ^ swzA64(r)) << 3);
  const unsigned short* bsrc = w2 + (size_t)r * DM + col0 + (((lane & 7) ^ swzB64(r)) << 3);
  auto issue = [&](int st) {
    char* buf = sm + (st & 7) * 16384;
    GLDS(asrc + st * 64, buf + wave * 1024);
    GLDS(bsrc + (size_t)st * 64 * DM, buf + 8192 + wave * 1024);
  };
#pragma unroll
  for (int s = 0; s < 7; ++s) issue(s);
  const unsigned kh = wave >> 2, wr = (wave >> 1) & 1, wc = wave & 1;
  f32x4 acc[2][2];
#pragma unroll
  for (int i = 0; i < 2; ++i)
#pragma unroll
    for (int j = 0; j < 2; ++j) acc[i][j] = f32x4{0.f, 0.f, 0.f, 0.f};
#pragma unroll 1
  for (int st = 0; st < NST; ++st) {
    if (st + 7 <= NST) WAITV(12); else WAITV(0);
    BARRIER();
    if (st + 7 < NST) issue(st + 7);
    const char* A = sm + (st & 7) * 16384;
    const char* Bc = A + 8192;
    unsigned r0 = wr * 32 + (lane & 15), r1 = r0 + 16, ch = 4 * kh + (lane >> 4);
    bf16x8 a0 = *reinterpret_cast<const bf16x8*>(A + offA64(r0, ch));
    bf16x8 a1 = *reinterpret_cast<const bf16x8*>(A + offA64(r1, ch));
    bf16x8 bq[2];
    bq[0] = ldB64(Bc, lane, wc * 2, kh); bq[1] = ldB64(Bc, lane, wc * 2 + 1, kh);
    asm volatile("s_waitcnt lgkmcnt(0)" ::: "memory"); __builtin_amdgcn_sched_barrier(0);
#pragma unroll
    for (int c = 0; c < 2; ++c) {
      bf16x8 bb = bq[c];
      acc[0][c] = __builtin_amdgcn_mfma_f32_16x16x32_bf16(a0, bb, acc[0][c], 0, 0, 0);
      acc[1][c] = __builtin_amdgcn_mfma_f32_16x16x32_bf16(a1, bb, acc[1][c], 0, 0, 0);
    }
  }
  __syncthreads();
  epi64(sm, acc, h, out, row0, col0);
}

extern "C" void kernel_launch(void* const* d_in, const int* in_sizes, int n_in, void* d_out, int out_size,
                              void* d_ws, size_t ws_size, hipStream_t stream) {
  const unsigned short* x = (const unsigned short*)d_in[0];
  const unsigned short* wqkv = (const unsigned short*)d_in[1];
  const unsigned short* wo = (const unsigned short*)d_in[2];
  const unsigned short* w1 = (const unsigned short*)d_in[4];
  const unsigned short* w2 = (const unsigned short*)d_in[5];
  unsigned short* out = (unsigned short*)d_out;

  char* ws = (char*)d_ws;
  unsigned short* q = (unsigned short*)(ws);
  unsigned short* hbuf = (unsigned short*)(ws + (2u << 20));
  unsigned short* h1 = (unsigned short*)(ws + (4u << 20));
  float* kvp = (float*)(ws + (12u << 20));
  float* zp = (float*)(ws + (13u << 20));
  unsigned short* wp = (unsigned short*)(ws + (14u << 20));
  float* z = (float*)(ws + (15u << 20));
  k1_qkv<<<256, 512, 0, stream>>>(x, wqkv, q, kvp, zp);
  k1b_fold<<<128, 256, 0, stream>>>(kvp, zp, wo, wp, z);
  k2_attn_out<<<256, 512, 0, stream>>>(q, z, wp, x, hbuf);
  k3_ffn1<<<256, 512, 0, stream>>>(hbuf, w1, h1);
  k4_ffn2<<<256, 512, 0, stream>>>(h1, w2, hbuf, out);
}
```
